```python
import torch

def _softmax(x, axis=-1):
    return torch.softmax(x, dim=axis)
def _gelu(x):
    return torch.nn.functional.gelu(x, approximate="tanh")
def _silu(x):
    return torch.nn.functional.silu(x)
def _sigmoid(x):
    return torch.sigmoid(x)
def _layernorm(x, axis=-1, eps=1e-5):
    m = x.mean(dim=axis, keepdim=True)
    v = x.var(dim=axis, keepdim=True, unbiased=False)
    return (x - m) / torch.sqrt(v + eps)
def _scatter_add(tgt, idx, src, axis=0):
    return tgt.clone().index_add_(axis, idx, src)
def _segment_sum(src, seg, n):
    out = torch.zeros((n,) + src.shape[1:], dtype=src.dtype)
    return out.index_add_(0, seg, src)
def _pad(x, pw):
    flat = tuple(v for pair in reversed(pw) for v in pair)
    return torch.nn.functional.pad(x, flat)

kernel_name = 'interleave_layers__spectral_gfnet__spectral_gfnet__prompt'

def setup_inputs():

    _w = (lambda a: (lambda t: t.to(torch.bfloat16) if t.is_floating_point() else t)(torch.as_tensor(a)))
    def _A_setup():
        B, S, D = 1, 2048, 1024
        Df = D // 2 + 1
        return {
            "x": _w((lambda *s: torch.randn(*s, dtype=torch.bfloat16) * 0.05)(B, S, D)),
            "w_r": _w((lambda *s: torch.randn(*s, dtype=torch.bfloat16) * 0.2)(S, Df) * 0.02),
            "w_i": _w((lambda *s: torch.randn(*s, dtype=torch.bfloat16) * 0.2)(S, Df) * 0.02),
        }
    _A_in = _A_setup()

    def _B_setup():
        B, S, D = 1, 2048, 1024
        Df = D // 2 + 1
        return {
            "x": _w((lambda *s: torch.randn(*s, dtype=torch.bfloat16) * 0.05)(B, S, D)),
            "w_r": _w((lambda *s: torch.randn(*s, dtype=torch.bfloat16) * 0.2)(S, Df) * 0.02),
            "w_i": _w((lambda *s: torch.randn(*s, dtype=torch.bfloat16) * 0.2)(S, Df) * 0.02),
        }
    _B_in = _B_setup()

    return {
        "A_x": _A_in["x"],
        "A_w_r": _A_in["w_r"],
        "A_w_i": _A_in["w_i"],
        "B_x": _B_in["x"],
        "B_w_r": _B_in["w_r"],
        "B_w_i": _B_in["w_i"],
    }

def reference(A_x, A_w_r, A_w_i, B_x, B_w_r, B_w_i):
    def _A_ref(x, w_r, w_i):
        B, S, D = x.shape
        X = (lambda x, axes=(-2,-1): torch.fft.rfft2(x.float(), dim=axes))(x, axes=(-2, -1))
        W = w_r + 1j * w_i
        X = X * W
        y = (lambda x, s, axes=(-2,-1): torch.fft.irfft2(x, s=s, dim=axes).to(torch.bfloat16))(X, (S, D), axes=(-2, -1))
        return y

    def _B_ref(x, w_r, w_i):
        B, S, D = x.shape
        X = (lambda x, axes=(-2,-1): torch.fft.rfft2(x.float(), dim=axes))(x, axes=(-2, -1))
        W = w_r + 1j * w_i
        X = X * W
        y = (lambda x, s, axes=(-2,-1): torch.fft.irfft2(x, s=s, dim=axes).to(torch.bfloat16))(X, (S, D), axes=(-2, -1))
        return y

    x = A_x
    x = x + _A_ref(_layernorm(x, axis=-1), A_w_r, A_w_i)
    x = x + _B_ref(_layernorm(x, axis=-1), B_w_r, B_w_i)
    x = x + _A_ref(_layernorm(x, axis=-1), A_w_r, A_w_i)
    x = x + _B_ref(_layernorm(x, axis=-1), B_w_r, B_w_i)
    return x
```

```cpp
#include <hip/hip_runtime.h>

typedef _Float16 h2 __attribute__((ext_vector_type(2)));
typedef _Float16 h8 __attribute__((ext_vector_type(8)));
typedef __bf16 b2 __attribute__((ext_vector_type(2)));
typedef __bf16 b8 __attribute__((ext_vector_type(8)));
typedef float f2 __attribute__((ext_vector_type(2)));
typedef float f4 __attribute__((ext_vector_type(4)));
typedef unsigned u2 __attribute__((ext_vector_type(2)));
typedef unsigned u4 __attribute__((ext_vector_type(4)));
typedef unsigned __attribute__((aligned(2))) u32a2;
typedef unsigned long long u64;

#define DEV __device__ __forceinline__

constexpr int NWG = 256, NT = 512;
constexpr int REP = 8;
constexpr unsigned SENT = 0xFFFFFFFFu;
constexpr unsigned SPIN_LIMIT = 1u << 17;
constexpr unsigned OFF_E = 0, OFF_F1 = 0x1000, OFF_F2 = 0x2000, OFF_F3 = 0x3000, OFF_S1 = 0x4000;
constexpr unsigned OFF_R = 0x10000, OFF_P = 0x100000, OFF_U1 = 0x400000, OFF_WA = 0x800000, OFF_WB = 0xC00000,
                   OFF_DZ = 0x1000000, WS_USED = 0x1400000;
constexpr int TILE_COL = 544, TILE_Q = 1088;
constexpr int L_TILE = 0;
constexpr int PITCH_A = 580, L_STA = 4352;
constexpr int L_WST = L_STA + 8 * PITCH_A;
constexpr int PITCH_B = 644, L_STB = L_STA;
constexpr int L_MISC = L_WST + 256 * 20;
constexpr int L_TOTAL = L_MISC + 64;
#define DLY_H1 10
#define DLY_H2 18
#define DLY_H2W 40
#define DLY_DZ 8

DEV h2 pack_h(float a, float b) { f2 v = {a, b}; return __builtin_convertvector(v, h2); }
DEV unsigned pack_b(float a, float b) { f2 v = {a, b}; return __builtin_bit_cast(unsigned, __builtin_convertvector(v, b2)); }
DEV unsigned asu(h2 v) { return __builtin_bit_cast(unsigned, v); }
DEV h2 ash(unsigned v) { return __builtin_bit_cast(h2, v); }
DEV float asf(unsigned v) { return __builtin_bit_cast(float, v); }
DEV unsigned asu(float v) { return __builtin_bit_cast(unsigned, v); }
DEV float bflo(unsigned v) { return asf(v << 16); }
DEV float bfhi(unsigned v) { return asf(v & 0xffff0000u); }
DEV h2 cmul(h2 a, h2 t) { h2 p = a.xx * t; h2 tt = {-t.y, t.x}; return __builtin_elementwise_fma(a.yy, tt, p); }
DEV h2 cmulconj(h2 a, h2 t) {
  h2 t1 = {t.x, -t.y}; h2 p = a.xx * t1; h2 tt = {-t.y, -t.x}; return __builtin_elementwise_fma(a.yy, tt, p);
}
DEV void radix4(h2 a0, h2 a1, h2 a2, h2 a3, h2& o0, h2& o1, h2& o2, h2& o3) {
  h2 t0 = a0 + a2, t1 = a0 - a2, t2 = a1 + a3, t3 = a1 - a3;
  o0 = t0 + t2; o2 = t0 - t2;
  o1 = t1 + (h2){t3.y, -t3.x};
  o3 = t1 + (h2){-t3.y, t3.x};
}
template <int DEN> DEV f2 wexp(int num) {
  float fr = (float)(num & (DEN - 1)) * (1.0f / DEN);
  return (f2){__builtin_amdgcn_cosf(fr), -__builtin_amdgcn_sinf(fr)};
}
DEV f4 mfma_h(u4 a, u4 b, f4 c) {
  return __builtin_amdgcn_mfma_f32_16x16x32_f16(__builtin_bit_cast(h8, a), __builtin_bit_cast(h8, b), c, 0, 0, 0);
}
DEV f4 mfma_b(u4 a, u4 b, f4 c) {
  return __builtin_amdgcn_mfma_f32_16x16x32_bf16(__builtin_bit_cast(b8, a), __builtin_bit_cast(b8, b), c, 0, 0, 0);
}
template <int CTRL> DEV unsigned dppu(unsigned x) { return (unsigned)__builtin_amdgcn_update_dpp(0, (int)x, CTRL, 0xf, 0xf, true); }
template <int CTRL> DEV float dppf(float x) { return asf(dppu<CTRL>(asu(x))); }
DEV u2 swap16(unsigned x) { unsigned y = x; asm volatile("" : "+v"(y)); auto r = __builtin_amdgcn_permlane16_swap(x, y, false, false); u2 o = {r[0], r[1]}; asm volatile("" : "+v"(o)); return o; }
DEV u2 swap32(unsigned x) { unsigned y = x; asm volatile("" : "+v"(y)); auto r = __builtin_amdgcn_permlane32_swap(x, y, false, false); u2 o = {r[0], r[1]}; asm volatile("" : "+v"(o)); return o; }
DEV float wave_sum(float x) {
  x += dppf<0xB1>(x); x += dppf<0x4E>(x); x += dppf<0x124>(x); x += dppf<0x128>(x);
  u2 a = swap16(asu(x)); x = asf(a.x) + asf(a.y);
  u2 b = swap32(asu(x)); x = asf(b.x) + asf(b.y);
  return x;
}
DEV unsigned pkmax(unsigned a, unsigned b) {
  typedef unsigned short s2 __attribute__((ext_vector_type(2)));
  return __builtin_bit_cast(unsigned, __builtin_elementwise_max(__builtin_bit_cast(s2, a), __builtin_bit_cast(s2, b)));
}
DEV unsigned wave_pkmax(unsigned x) {
  x = pkmax(x, dppu<0xB1>(x)); x = pkmax(x, dppu<0x4E>(x)); x = pkmax(x, dppu<0x124>(x)); x = pkmax(x, dppu<0x128>(x));
  u2 a = swap16(x); x = pkmax(a.x, a.y);
  u2 b = swap32(x); x = pkmax(b.x, b.y);
  return x;
}
DEV unsigned umax(unsigned a, unsigned b) { return a > b ? a : b; }
DEV unsigned umin(unsigned a, unsigned b) { return a < b ? a : b; }
DEV unsigned max4(u4 v) { return umax(umax(v.x, v.y), umax(v.z, v.w)); }
DEV unsigned or4(u4 v) { return (v.x | v.y) | (v.z | v.w); }
DEV u4 sanit(u4 v) { return (u4){umin(v.x, SENT - 1), umin(v.y, SENT - 1), umin(v.z, SENT - 1), umin(v.w, SENT - 1)}; }

typedef __amdgpu_buffer_rsrc_t rsrc_t;
DEV u4 ld16(rsrc_t r, unsigned off) { return __builtin_amdgcn_raw_buffer_load_b128(r, off, 0, 16); }
DEV u2 ld8(rsrc_t r, unsigned off) { return __builtin_amdgcn_raw_buffer_load_b64(r, off, 0, 16); }
DEV void st16(rsrc_t r, unsigned off, u4 v) { __builtin_amdgcn_raw_buffer_store_b128(v, r, off, 0, 16); }
DEV void st8(rsrc_t r, unsigned off, u2 v) { __builtin_amdgcn_raw_buffer_store_b64(v, r, off, 0, 16); }
#define MEMBAR() asm volatile("" ::: "memory")
#define DRAIN() asm volatile("s_waitcnt vmcnt(0)" ::: "memory")
template <int N> DEV void snooze() {
  if constexpr (N > 0) __builtin_amdgcn_s_sleep(N);
}
DEV void pin(u4& v) { asm volatile("" : "+v"(v)); }
DEV void pin(unsigned& v) { asm volatile("" : "+v"(v)); }
DEV void pin(float& v) { asm volatile("" : "+v"(v)); }
DEV unsigned frag_re(h2 f) { return asu((h2){f.x, -f.y}); }
DEV unsigned frag_im(h2 f) { return asu((h2){f.y, f.x}); }

DEV int pos_to_n(int pi) {
  int n2 = pi >> 4;
  int gq = ((pi >> 2) & 3) ^ ((n2 >> 1) & 3);
  return 32 * (4 * gq + (pi & 3)) + n2;
}

struct WRaw { unsigned r[2][2], i[2][2]; };
DEV WRaw w_load(const unsigned short* wr, const unsigned short* wi, int p, int t) {
  WRaw o;
  const int q = p >> 6, g = (p >> 4) & 3, k1 = p & 15;
  const int c4 = t & 3;
  const int row0 = q + 4 * k1 + 64 * (16 * (c4 >> 1) + 4 * g + 2 * (c4 & 1));
#pragma unroll
  for (int h = 0; h < 2; ++h) {
    const int j = (t >> 2) + 128 * h;
    const int e = row0 * 513 + 2 * j + 1;
    o.r[h][0] = *(const u32a2*)(wr + e); o.r[h][1] = *(const u32a2*)(wr + e + 64 * 513);
    o.i[h][0] = *(const u32a2*)(wi + e); o.i[h][1] = *(const u32a2*)(wi + e + 64 * 513);
  }
  return o;
}
DEV void w_publish(const WRaw& w, rsrc_t ws, unsigned base, int p, int t) {
#pragma unroll
  for (int h = 0; h < 2; ++h) {
    const int j = (t >> 2) + 128 * h;
    u4 v;
    v.x = __builtin_amdgcn_perm(w.i[h][0], w.r[h][0], 0x05040100u);
    v.y = __builtin_amdgcn_perm(w.i[h][1], w.r[h][1], 0x05040100u);
    v.z = __builtin_amdgcn_perm(w.i[h][0], w.r[h][0], 0x07060302u);
    v.w = __builtin_amdgcn_perm(w.i[h][1], w.r[h][1], 0x07060302u);
    st16(ws, base + j * 16384 + p * 64 + (t & 3) * 16, v);
  }
}

#define POLL_LOOP(LOADS, OKEXPR)                                   \
  for (unsigned spins_ = 0;;) {                                    \
    LOADS;                                                         \
    if (__all(OKEXPR)) break;                                      \
    if (++spins_ > spin_limit) { spin_limit = 0; break; }          \
    MEMBAR();                                                      \
  }

__global__ void __launch_bounds__(NT, 2)
gfnet_kernel(const unsigned short* x, const unsigned short* awr, const unsigned short* awi, const unsigned short* bwr,
             const unsigned short* bwi, unsigned short* out, unsigned char* wsp) {
  __shared__ __attribute__((aligned(16))) unsigned lds[L_TOTAL];
  const int t = threadIdx.x, lane = t & 63;
  const int wave = __builtin_amdgcn_readfirstlane(t >> 6);
  const int wg = blockIdx.x;
  const int l15 = lane & 15, g = lane >> 4;
  const rsrc_t ws = __builtin_amdgcn_make_buffer_rsrc(wsp, 0, WS_USED, 0x00020000);
  unsigned spin_limit = SPIN_LIMIT;
  const u4 sent4 = {SENT, SENT, SENT, SENT};

  const int g2 = (wg >> 3) & 3, sidx = (wg & 7) + 8 * (wg >> 5);
  st16(ws, OFF_U1 + wg * 16384 + t * 16, sent4);
  st16(ws, OFF_U1 + wg * 16384 + 8192 + t * 16, sent4);
  {
    unsigned off = OFF_P + (sidx * 4 + g2) * 4096 + t * 16;
    if (t >= 256) off = OFF_R + (((t >> 3) & 7) * 4 + g2) * 8192 + sidx * 128 + (t & 7) * 16;
    if (t >= 320) off = OFF_S1 + wg * 32 + (t & 1) * 16;
    if (t < 322) st16(ws, off, sent4);
  }
  MEMBAR();
  u2 epoch = *(const u2*)(wsp + OFF_E);
  MEMBAR();
  const int pi0 = 4 * (wg & 127) + (wg >> 7);
  const int row = pos_to_n(pi0 + 2 * (wave >> 2)) + 512 * (wave & 3);
  unsigned xr[8];
  {
    const unsigned* xp = (const unsigned*)(x + row * 1024) + 128 * g + l15;
#pragma unroll
    for (int j = 0; j < 8; ++j) xr[j] = xp[16 * j];
  }
  WRaw wa = w_load(awr, awi, wg, t);
  WRaw wb = w_load(bwr, bwi, wg, t);
  MEMBAR();
  asm volatile("s_waitcnt vmcnt(24)" : "+v"(epoch) :: "memory");
  const u64 tag = (((u64)epoch.y << 32) | epoch.x) + 1;
  __builtin_amdgcn_s_barrier();
  if (t == 0) st8(ws, OFF_F1 + wg * 8, (u2){(unsigned)tag, (unsigned)(tag >> 32)});

  u4 ma1[2][2], ma2[2][2];
  unsigned ta[2][2][4];
  {
    float cr[8], ci[8];
#pragma unroll
    for (int j = 0; j < 8; ++j) { f2 w = wexp<32>((8 * g + j) * l15); cr[j] = w.x * 0.25f; ci[j] = w.y * 0.25f; }
#pragma unroll
    for (int j = 0; j < 4; ++j) {
      ma1[0][0][j] = asu(pack_h(cr[2 * j], cr[2 * j + 1])); ma1[0][1][j] = asu(pack_h(ci[2 * j], ci[2 * j + 1]));
      ma1[1][0][j] = ma1[0][0][j] ^ 0x80000000u; ma1[1][1][j] = ma1[0][1][j] ^ 0x80000000u;
    }
#pragma unroll
    for (int e = 0; e < 2; ++e)
#pragma unroll
      for (int i = 0; i < 4; ++i) {
        const int b = 2 * (4 * g + i) + e;
        f2 w = wexp<32>(b * l15);
        h2 f = pack_h(w.x * 0.125f, w.y * 0.125f);
        ma2[e][0][i] = frag_re(f); ma2[e][1][i] = frag_im(f);
#pragma unroll
        for (int k = 0; k < 2; ++k) { f2 v = wexp<1024>(b * (16 * k + l15)); ta[e][k][i] = asu(pack_h(v.x, v.y)); pin(ta[e][k][i]); }
      }
#pragma unroll
    for (int e = 0; e < 2; ++e) { pin(ma1[e][0]); pin(ma1[e][1]); pin(ma2[e][0]); pin(ma2[e][1]); }
  }
  {
    const float x0 = bflo(__builtin_amdgcn_readfirstlane(xr[0]));
    float d[2][8], se = 0.f, so = 0.f, qq = 0.f;
#pragma unroll
    for (int j = 0; j < 8; ++j) {
      d[0][j] = bflo(xr[j]) - x0; d[1][j] = bfhi(xr[j]) - x0;
      se += d[0][j]; so += d[1][j];
      qq = __builtin_fmaf(d[0][j], d[0][j], qq); qq = __builtin_fmaf(d[1][j], d[1][j], qq);
    }
    se = wave_sum(se); so = wave_sum(so); qq = wave_sum(qq);
    const float md = (se + so) * (1.0f / 1024);
    const float var = __builtin_fmaxf(__builtin_fmaf(-md, md, qq * (1.0f / 1024)), 0.f) + 1e-5f;
    const float r = __builtin_amdgcn_rsqf(var);
    const float off = -md * r;
    u4 xa[2];
#pragma unroll
    for (int e = 0; e < 2; ++e)
#pragma unroll
      for (int j = 0; j < 4; ++j)
        xa[e][j] = asu(pack_h(__builtin_fmaf(d[e][2 * j], r, off), __builtin_fmaf(d[e][2 * j + 1], r, off)));
    const f4 z4 = {0.f, 0.f, 0.f, 0.f};
    u4 hh[2][2];
#pragma unroll
    for (int k = 0; k < 2; ++k)
#pragma unroll
      for (int e = 0; e < 2; ++e) {
        f4 dr = mfma_h(xa[e], ma1[k][0], z4), di = mfma_h(xa[e], ma1[k][1], z4);
#pragma unroll
        for (int i = 0; i < 4; ++i) hh[e][k][i] = asu(cmul(pack_h(dr[i], di[i]), ash(ta[e][k][i])));
      }
#pragma unroll
    for (int k = 0; k < 2; ++k) {
      f4 orr = mfma_h(hh[0][k], ma2[0][0], z4), oi = mfma_h(hh[0][k], ma2[0][1], z4);
      orr = mfma_h(hh[1][k], ma2[1][0], orr); oi = mfma_h(hh[1][k], ma2[1][1], oi);
      u4 o;
#pragma unroll
      for (int i = 0; i < 4; ++i) o[i] = asu(pack_h(orr[i], oi[i]));
      *(u4*)&lds[L_STA + wave * PITCH_A + 36 * l15 + 16 * k + 4 * g] = o;
    }
    if (lane == 0) {
      lds[L_STA + wave * PITCH_A + 576] = asu(pack_h((se - so) * r * (0.70710678f / 32), 0.f));
      lds[L_MISC + wave] = asu(var * r) & 0x7fffffffu;
    }
  }
  w_publish(wa, ws, OFF_WA, wg, t);
  if (wave == 7) {
    u4 a, b;
    POLL_LOOP(a = ld16(ws, OFF_F1 + lane * 32); b = ld16(ws, OFF_F1 + lane * 32 + 16),
              a.x == (unsigned)tag && a.z == (unsigned)tag && b.x == (unsigned)tag && b.z == (unsigned)tag &&
              a.y == (unsigned)(tag >> 32) && a.w == (unsigned)(tag >> 32) && b.y == (unsigned)(tag >> 32) && b.w == (unsigned)(tag >> 32));
  }
  __syncthreads();
  if (t < 2) st16(ws, OFF_S1 + wg * 32 + t * 16, *(const u4*)&lds[L_MISC + 4 * t]);
  MEMBAR();
#pragma unroll
  for (int h = 0; h < 2; ++h) {
    const int j = (t >> 2) + 128 * h;
    const int k = 2 * j + 1 + (t & 1);
    const unsigned* src = &lds[L_STA + ((t >> 1) & 1) * 4 * PITCH_A + k + 4 * (k >> 5)];
    u4 v = {src[0], src[PITCH_A], src[2 * PITCH_A], src[3 * PITCH_A]};
    st16(ws, OFF_U1 + j * 16384 + wg * 64 + (t & 3) * 16, sanit(v));
  }
  MEMBAR();
  asm volatile("s_waitcnt vmcnt(2)" ::: "memory");
  __builtin_amdgcn_s_barrier();
  if (t == 0) st8(ws, OFF_F2 + wg * 8, (u2){(unsigned)tag, (unsigned)(tag >> 32)});

  const int q = wave & 3, wcol = wave >> 2;
  u4 m1[2], m3[2], m2[2][2][2];
  unsigned t1[2][4], t2[2][4];
#pragma unroll
  for (int i = 0; i < 4; ++i) {
    { f2 w = wexp<64>((4 * g + i) * (q + 4 * l15)); h2 f = pack_h(w.x * 0.25f, w.y * 0.25f); m1[0][i] = frag_re(f); m1[1][i] = frag_im(f); }
    { f2 w = wexp<64>(l15 * (q + 4 * (4 * g + i))); h2 f = pack_h(w.x * 0.25f, w.y * 0.25f); m3[0][i] = frag_re(f); m3[1][i] = frag_im(f); }
    {
      f2 w = wexp<32>(l15 * (4 * g + i));
      h2 f = pack_h(w.x * 0.125f, w.y * 0.125f);
      const unsigned fr = frag_re(f), fi = frag_im(f);
      const unsigned sl = (l15 & 1) ? 0x80008000u : 0u;
      const unsigned si = (i & 1) ? 0x80008000u : 0u;
      m2[0][0][0][i] = fr; m2[0][0][1][i] = fi;
      m2[0][1][0][i] = fr ^ sl; m2[0][1][1][i] = fi ^ sl;
      m2[1][0][0][i] = fr ^ si; m2[1][0][1][i] = fi ^ si;
      m2[1][1][0][i] = fr ^ sl ^ si; m2[1][1][1][i] = fi ^ sl ^ si;
    }
#pragma unroll
    for (int b = 0; b < 2; ++b) {
      { f2 w = wexp<2048>((16 * b + 4 * g + i) * (q + 4 * l15)); t1[b][i] = asu(pack_h(w.x, w.y)); }
      { f2 w = wexp<2048>((16 * b + l15) * (q + 4 * (4 * g + i))); t2[b][i] = asu(pack_h(w.x, w.y)); }
      pin(t1[b][i]); pin(t2[b][i]);
    }
  }
#pragma unroll
  for (int e = 0; e < 2; ++e) {
    pin(m1[e]); pin(m3[e]); pin(m2[0][0][e]); pin(m2[0][1][e]); pin(m2[1][0][e]); pin(m2[1][1][e]);
  }
  const u4 redA = {(l15 & 7) == 0 ? 0x3F80u : (l15 & 7) == 1 ? 0x3F800000u : 0u, (l15 & 7) == 2 ? 0x3F80u : (l15 & 7) == 3 ? 0x3F800000u : 0u,
                   (l15 & 7) == 4 ? 0x3F80u : (l15 & 7) == 5 ? 0x3F800000u : 0u, (l15 & 7) == 6 ? 0x3F80u : (l15 & 7) == 7 ? 0x3F800000u : 0u};
  unsigned* const own = &lds[L_TILE + (t & 1) * TILE_COL + (t >> 1) * 2];
  unsigned* const tile = &lds[L_TILE + q * TILE_Q + wcol * TILE_COL + l15 * 16 + 4 * (g ^ ((l15 >> 1) & 3))];
  const bool nyq = (wg == NWG - 1) && (t & 1);

  auto w_fetch = [&](unsigned fbase, unsigned wbase, int slot) {
    u2 f;
    POLL_LOOP(f = ld8(ws, fbase + (wave * 16 + (lane & 15) + 128 * ((lane >> 4) & 1)) * 8),
              f.x == (unsigned)tag && f.y == (unsigned)(tag >> 32));
    MEMBAR();
    u4 a = ld16(ws, wbase + wg * 16384 + t * 16), b = ld16(ws, wbase + wg * 16384 + 8192 + t * 16);
    *(u4*)&lds[L_WST + (t >> 2) * 20 + (t & 3) * 4] = a;
    *(u4*)&lds[L_WST + (128 + (t >> 2)) * 20 + (t & 3) * 4] = b;
    const unsigned M = 0x7fff7fffu;
    unsigned c0 = pkmax(pkmax(a.x & M, a.y & M), pkmax(b.x & M, b.y & M));
    unsigned c1 = pkmax(pkmax(a.z & M, a.w & M), pkmax(b.z & M, b.w & M));
    c0 = umax(c0 & 0xffffu, c0 >> 16); c1 = umax(c1 & 0xffffu, c1 >> 16);
    unsigned m = wave_pkmax(c0 | (c1 << 16));
    if (lane == 0) lds[L_MISC + 16 + slot * 8 + wave] = m;
  };
  auto w_convert = [&](int slot, unsigned (&wv)[2][4], float& unscale) {
    u4 ma = *(const u4*)&lds[L_MISC + 16 + slot * 8], mb = *(const u4*)&lds[L_MISC + 16 + slot * 8 + 4];
    unsigned m = pkmax(pkmax(pkmax(ma.x, ma.y), pkmax(ma.z, ma.w)), pkmax(pkmax(mb.x, mb.y), pkmax(mb.z, mb.w)));
    unsigned e0 = umin(umax((m & 0xffffu) >> 7, 4u), 254u), e1 = umin(umax(m >> 23, 4u), 254u);
    const float sc = asf((256u - (wcol ? e1 : e0)) << 23);
    unscale = asf((((t & 1) ? e1 : e0) - 3u) << 23);
    const unsigned* src = &lds[L_WST + (q * 64 + lane) * 20 + wcol * 2];
#pragma unroll
    for (int c = 0; c < 4; ++c) {
      u2 v = *(const u2*)&src[4 * c];
      wv[c >> 1][2 * (c & 1)] = asu(pack_h(bflo(v.x) * sc, bfhi(v.x) * sc));
      wv[c >> 1][2 * (c & 1) + 1] = asu(pack_h(bflo(v.y) * sc, bfhi(v.y) * sc));
    }
  };

  w_fetch(OFF_F2, OFF_WA, 0);
  u4 ua, ub;
  POLL_LOOP(ua = ld16(ws, OFF_U1 + wg * 16384 + t * 16); ub = ld16(ws, OFF_U1 + wg * 16384 + 8192 + t * 16),
            umax(max4(ua), max4(ub)) != SENT);
  if (wg == 0 && t == 0) *(u64*)(wsp + OFF_E) = tag;

  unsigned wva[2][4], wvb[2][4];
  float usa = 0.f, usb = 0.f;
  float zr[2][4], zi[2][4];
  u4 xres[2];
  u4 mb1[2][2], mb2[2][2];
  float tbr[2][2][4], tbi[2][2][4];
  f4 sig;

#pragma unroll
  for (int L = 0; L < 4; ++L) {
    h2 c[2][4];
    if (L == 0) {
      radix4(ash(ua.x), ash(ua.y), ash(ua.z), ash(ua.w), c[0][0], c[0][1], c[0][2], c[0][3]);
      radix4(ash(ub.x), ash(ub.y), ash(ub.z), ash(ub.w), c[1][0], c[1][1], c[1][2], c[1][3]);
    } else {
      u4 v[4];
      const unsigned rb = OFF_R + (((L - 1) * REP + (wg & (REP - 1))) * 4) * 8192 + t * 16;
      POLL_LOOP(v[0] = ld16(ws, rb); v[1] = ld16(ws, rb + 8192); v[2] = ld16(ws, rb + 16384); v[3] = ld16(ws, rb + 24576),
                (int)(or4(v[0]) | or4(v[1]) | or4(v[2]) | or4(v[3])) >= 0);
      h2 u[2][4];
#pragma unroll
      for (int m = 0; m < 4; ++m) {
        const float sum = (asf(v[0][m]) + asf(v[1][m])) + (asf(v[2][m]) + asf(v[3][m]));
        const float r = __builtin_amdgcn_rsqf(__builtin_fmaf(sum, 2.0f / 1024, 1e-5f));
        const float ra = dppf<0xA0>(r), rbb = dppf<0xF5>(r);
        u[0][m] = pack_h(zr[0][m] * ra, zi[0][m] * ra);
        u[1][m] = pack_h(zr[1][m] * rbb, zi[1][m] * rbb);
      }
      radix4(u[0][0], u[0][1], u[0][2], u[0][3], c[0][0], c[0][1], c[0][2], c[0][3]);
      radix4(u[1][0], u[1][1], u[1][2], u[1][3], c[1][0], c[1][1], c[1][2], c[1][3]);
    }
#pragma unroll
    for (int k = 0; k < 4; ++k) *(u2*)&own[k * TILE_Q] = (u2){asu(c[0][k]), asu(c[1][k])};
    __syncthreads();
    if (L == 0) {
      w_publish(wb, ws, OFF_WB, wg, t);
      st16(ws, OFF_DZ + wg * 16384 + t * 16, sent4);
      st16(ws, OFF_DZ + wg * 16384 + 8192 + t * 16, sent4);
      st16(ws, OFF_P + ((1 + (t >> 8)) * 256 + sidx * 4 + g2) * 4096 + (t & 255) * 16, sent4);
      if (t < 128) st16(ws, OFF_R + (((1 + (t >> 6)) * REP + ((t >> 3) & 7)) * 4 + g2) * 8192 + sidx * 128 + (t & 7) * 16, sent4);
      w_convert(0, wva, usa);
    }
    if (L == 1) w_convert(1, wvb, usb);
    {
      const f4 z4 = {0.f, 0.f, 0.f, 0.f};
      u4 hh[2], vv[2];
#pragma unroll
      for (int b = 0; b < 2; ++b) {
        const u4 a = *(const u4*)&tile[b * 256];
        f4 dr = mfma_h(a, m1[0], z4), di = mfma_h(a, m1[1], z4);
#pragma unroll
        for (int i = 0; i < 4; ++i) hh[b][i] = asu(cmul(pack_h(dr[i], di[i]), ash(t1[b][i])));
      }
#pragma unroll
      for (int k = 0; k < 2; ++k) {
        f4 xr_ = mfma_h(m2[k][0][0], hh[0], z4), xi_ = mfma_h(m2[k][0][1], hh[0], z4);
        xr_ = mfma_h(m2[k][1][0], hh[1], xr_); xi_ = mfma_h(m2[k][1][1], hh[1], xi_);
#pragma unroll
        for (int i = 0; i < 4; ++i) vv[k][i] = asu(cmulconj(pack_h(xr_[i], xi_[i]), ash((L & 1) ? wvb[k][i] : wva[k][i])));
      }
#pragma unroll
      for (int b = 0; b < 2; ++b) {
        f4 gr = mfma_h(vv[0], m2[b][0][0], z4), gi = mfma_h(vv[0], m2[b][0][1], z4);
        gr = mfma_h(vv[1], m2[b][1][0], gr); gi = mfma_h(vv[1], m2[b][1][1], gi);
        u4 gg;
#pragma unroll
        for (int i = 0; i < 4; ++i) gg[i] = asu(cmul(pack_h(gr[i], gi[i]), ash(t2[b][i])));
        f4 er = mfma_h(m3[0], gg, z4), ei = mfma_h(m3[1], gg, z4);
        u4 o;
#pragma unroll
        for (int i = 0; i < 4; ++i) o[i] = asu(pack_h(er[i], ei[i]));
        *(u4*)&tile[b * 256] = o;
      }
    }
    if (L == 0) {
      u4 s;
      const int pi = t;
      POLL_LOOP(s = ld16(ws, OFF_S1 + ((pi & 1) * 128 + (pi >> 2)) * 32 + ((pi >> 1) & 1) * 16), (int)or4(s) >= 0);
      sig = __builtin_bit_cast(f4, s);
#pragma unroll
      for (int m = 0; m < 4; ++m) {
        const float sa = dppf<0xA0>(sig[m]), sb = dppf<0xF5>(sig[m]);
        zr[0][m] = (float)ash(ua[m]).x * sa; zi[0][m] = (float)ash(ua[m]).y * sa;
        zr[1][m] = (float)ash(ub[m]).x * sb; zi[1][m] = (float)ash(ub[m]).y * sb;
      }
    }
    __syncthreads();
    {
      u2 e[4];
#pragma unroll
      for (int k = 0; k < 4; ++k) e[k] = *(const u2*)&own[k * TILE_Q];
      h2 y[2][4];
      radix4(ash(e[0].x), ash(e[1].x), ash(e[2].x), ash(e[3].x), y[0][0], y[0][1], y[0][2], y[0][3]);
      radix4(ash(e[0].y), ash(e[1].y), ash(e[2].y), ash(e[3].y), y[1][0], y[1][1], y[1][2], y[1][3]);
      const float us = (L & 1) ? usb : usa;
      const float usi = nyq ? 0.f : -us;
#pragma unroll
      for (int s = 0; s < 2; ++s)
#pragma unroll
        for (int m = 0; m < 4; ++m) {
          zr[s][m] = __builtin_fmaf((float)y[s][m].x, us, zr[s][m]);
          zi[s][m] = __builtin_fmaf((float)y[s][m].y, usi, zi[s][m]);
        }
    }
    if (L < 3) {
      float p[2][4];
#pragma unroll
      for (int s = 0; s < 2; ++s)
#pragma unroll
        for (int m = 0; m < 4; ++m) {
          const float sq = __builtin_fmaf(zr[s][m], zr[s][m], zi[s][m] * zi[s][m]);
          p[s][m] = sq + dppf<0xB1>(sq);
        }
      const unsigned M = 0x7fff7fffu;
      u4 pv = {pack_b(p[0][0], p[0][1]) & M, pack_b(p[0][2], p[0][3]) & M, pack_b(p[1][0], p[1][1]) & M, pack_b(p[1][2], p[1][3]) & M};
      if (L == 0) {
        DRAIN();
        __builtin_amdgcn_s_barrier();
        if (t == 0) st8(ws, OFF_F3 + wg * 8, (u2){(unsigned)tag, (unsigned)(tag >> 32)});
      }
      if (!(t & 1)) st16(ws, OFF_P + (L * 256 + (t >> 3) * 4 + g2) * 4096 + sidx * 64 + ((t >> 1) & 3) * 16, pv);
      MEMBAR();
      if (wave == 0) {
        snooze<DLY_H1>();
        u4 v[4];
        const unsigned pb = OFF_P + (L * 256 + sidx * 4 + g2) * 4096 + lane * 16;
        POLL_LOOP(v[0] = ld16(ws, pb); v[1] = ld16(ws, pb + 1024); v[2] = ld16(ws, pb + 2048); v[3] = ld16(ws, pb + 3072),
                  ((or4(v[0]) | or4(v[1]) | or4(v[2]) | or4(v[3])) & 0x80008000u) == 0);
        f4 acc = {0.f, 0.f, 0.f, 0.f};
#pragma unroll
        for (int i = 0; i < 4; ++i) acc = mfma_b(redA, v[i], acc);
        u4 o;
#pragma unroll
        for (int i = 0; i < 4; ++i) { float s = acc[i]; s += dppf<0x124>(s); s += dppf<0x128>(s); o[i] = asu(s) & 0x7fffffffu; }
        st16(ws, OFF_R + ((L * REP + (l15 >> 2) + 4 * (g >> 1)) * 4 + g2) * 8192 + sidx * 128 + (l15 & 3) * 32 + (g & 1) * 16, o);
        MEMBAR();
      }
      if (L == 0) {
        if (wave != 0) snooze<DLY_H1 + 14>();
        w_fetch(OFF_F3, OFF_WB, 1);
        snooze<4>();
      } else if (L == 2) {
        const u4* xp = (const u4*)(x + row * 1024) + l15 * 4 + g;
        xres[0] = xp[0]; xres[1] = xp[64];
#pragma unroll
        for (int i = 0; i < 4; ++i) {
#pragma unroll
          for (int b = 0; b < 2; ++b) {
            const int bb = 8 * (l15 >> 2) + 4 * b + (l15 & 3);
            f2 w = wexp<32>(bb * (4 * g + i));
            mb1[b][0][i] = pack_b(w.x * 0.25f, w.y * 0.25f);
            mb1[b][1][i] = pack_b(-w.y * 0.25f, w.x * 0.25f);
#pragma unroll
            for (int k = 0; k < 2; ++k) {
              f2 v = wexp<1024>(bb * (16 * k + 4 * g + i)); tbr[k][b][i] = v.x; tbi[k][b][i] = -v.y;
              pin(tbr[k][b][i]); pin(tbi[k][b][i]);
            }
          }
#pragma unroll
          for (int k = 0; k < 2; ++k) {
            f2 w = wexp<32>(l15 * (16 * k + 4 * g + i));
            mb2[0][k][i] = pack_b(w.x * 0.25f, w.y * 0.25f);
            mb2[1][k][i] = mb2[0][k][i] ^ ((i & 1) ? 0x80008000u : 0u);
          }
        }
#pragma unroll
        for (int e = 0; e < 2; ++e) { pin(mb1[e][0]); pin(mb1[e][1]); pin(mb2[e][0]); pin(mb2[e][1]); }
        if (wave == 0) snooze<4>(); else snooze<DLY_H2W - 22>();
      } else {
        if (wave == 0) snooze<DLY_H2>(); else snooze<DLY_H2W>();
      }
    }
  }
  {
    u4 da, db;
#pragma unroll
    for (int m = 0; m < 4; ++m) {
      const float sa = dppf<0xA0>(sig[m]), sb = dppf<0xF5>(sig[m]);
      da[m] = pack_b(__builtin_fmaf(-(float)ash(ua[m]).x, sa, zr[0][m]), __builtin_fmaf(-(float)ash(ua[m]).y, sa, zi[0][m]));
      db[m] = pack_b(__builtin_fmaf(-(float)ash(ub[m]).x, sb, zr[1][m]), __builtin_fmaf(-(float)ash(ub[m]).y, sb, zi[1][m]));
    }
    st16(ws, OFF_DZ + (t >> 2) * 16384 + wg * 64 + (t & 3) * 16, sanit(da));
    st16(ws, OFF_DZ + (128 + (t >> 2)) * 16384 + wg * 64 + (t & 3) * 16, sanit(db));
    MEMBAR();
  }
  {
    if (lane == 0) lds[L_STB + wave * PITCH_B] = 0u;
    snooze<DLY_DZ>();
    const int qd = lane >> 2;
    const int j0 = 16 * ((qd >> 1) & 3) + (qd & 1) + 2 * (qd >> 3) + 4 * (wave & 3) + 64 * (wave >> 2);
    u4 v[2];
    const unsigned zb = OFF_DZ + wg * 16384 + j0 * 64 + (t & 3) * 16;
    POLL_LOOP(v[0] = ld16(ws, zb); v[1] = ld16(ws, zb + 8192), umax(max4(v[0]), max4(v[1])) != SENT);
#pragma unroll
    for (int h = 0; h < 2; ++h) {
      const int k = 2 * (j0 + 128 * h) + 1 + (t & 1);
      const int pos = (k == 512) ? 640 : (k & 31) * 20 + (k >> 5);
      unsigned* dst = &lds[L_STB + ((t >> 1) & 1) * 4 * PITCH_B + pos];
      dst[0] = v[h].x; dst[PITCH_B] = v[h].y; dst[2 * PITCH_B] = v[h].z; dst[3 * PITCH_B] = v[h].w;
    }
    __syncthreads();
    const f4 z4 = {0.f, 0.f, 0.f, 0.f};
    const unsigned* src = &lds[L_STB + wave * PITCH_B];
    const float ny = bflo(src[640]) * (1.41421356f / 32);
    u4 gg[2][2];
#pragma unroll
    for (int k = 0; k < 2; ++k) {
      const u4 a = *(const u4*)&src[(16 * k + l15) * 20 + 4 * g];
#pragma unroll
      for (int b = 0; b < 2; ++b) {
        f4 gr = mfma_b(a, mb1[b][0], z4), gi = mfma_b(a, mb1[b][1], z4);
#pragma unroll
        for (int i = 0; i < 4; ++i) {
          const float cr = tbr[k][b][i], ci = tbi[k][b][i];
          gg[k][b][i] = pack_b(__builtin_fmaf(gr[i], cr, -gi[i] * ci), __builtin_fmaf(gr[i], ci, gi[i] * cr));
        }
      }
    }
    const f4 c0 = {ny, -ny, ny, -ny};
#pragma unroll
    for (int a = 0; a < 2; ++a) {
      u4 o;
#pragma unroll
      for (int b = 0; b < 2; ++b) {
        f4 y = mfma_b(gg[0][b], mb2[a][0], c0);
        y = mfma_b(gg[1][b], mb2[a][1], y);
        o[2 * b] = pack_b(bflo(xres[a][2 * b]) + y[0], bfhi(xres[a][2 * b]) + y[1]);
        o[2 * b + 1] = pack_b(bflo(xres[a][2 * b + 1]) + y[2], bfhi(xres[a][2 * b + 1]) + y[3]);
      }
      ((u4*)(out + row * 1024))[a * 64 + l15 * 4 + g] = o;
    }
  }
}

extern "C" void kernel_launch(void* const* d_in, const int* in_sizes, int n_in, void* d_out, int out_size, void* d_ws,
                              size_t ws_size, hipStream_t stream) {
  gfnet_kernel<<<NWG, NT, 0, stream>>>((const unsigned short*)d_in[0], (const unsigned short*)d_in[1],
                                       (const unsigned short*)d_in[2], (const unsigned short*)d_in[4],
                                       (const unsigned short*)d_in[5], (unsigned short*)d_out, (unsigned char*)d_ws);
}
```

```cpp
#include <hip/hip_runtime.h>

typedef _Float16 h2 __attribute__((ext_vector_type(2)));
typedef _Float16 h8 __attribute__((ext_vector_type(8)));
typedef __bf16 b2 __attribute__((ext_vector_type(2)));
typedef __bf16 b8 __attribute__((ext_vector_type(8)));
typedef float f2 __attribute__((ext_vector_type(2)));
typedef float f4 __attribute__((ext_vector_type(4)));
typedef unsigned u2 __attribute__((ext_vector_type(2)));
typedef unsigned u4 __attribute__((ext_vector_type(4)));
typedef u4 __attribute__((aligned(2))) u4a2;
typedef short s4 __attribute__((ext_vector_type(4)));
#define LAS __attribute__((address_space(3)))
typedef unsigned long long u64;

#define DEV __device__ __forceinline__

constexpr int NWG = 256, NT = 512;
constexpr int REP = 8;
constexpr unsigned SENT = 0xFFFFFFFFu;
constexpr unsigned SPIN_LIMIT = 1u << 17;
constexpr unsigned OFF_E = 0, OFF_R = 0x20000, OFF_P = 0x100000, OFF_U1 = 0x400000, OFF_WA = 0x800000, OFF_WB = 0xC00000;
constexpr unsigned FST = 32;
constexpr unsigned OFF_F1 = 0xE0000, OFF_F3 = 0xF0000;
constexpr unsigned OFF_S1 = 0x10000, WS_USED = 0x1000000;
constexpr int TILE_COL = 544, TILE_Q = 1088;
constexpr int L_TILE = 0;
constexpr int PITCH_A = 580, PITCH_B = 644;
constexpr int L_ST = 4352;
constexpr int PITCH_W = 68;
constexpr int L_WP = L_ST + 8 * PITCH_B;
constexpr int L_WST = L_WP + 64 * PITCH_W;
constexpr int L_CT = L_WST + 256 * 20;
constexpr int L_CL = L_CT + 14 * 256;
constexpr int L_MISC = L_CL + 32 * 256;
constexpr int L_X = L_MISC + 64;
constexpr int L_TOTAL = L_X + 8 * 512;
struct Dly { int u1, h1, h2, h1b, dz, wb, s0, s1, b2; };
constexpr Dly DLY = {480, 13, 10, 7, 10, 8, 12, 2, 140};

DEV h2 pack_h(float a, float b) { f2 v = {a, b}; return __builtin_convertvector(v, h2); }
DEV unsigned pack_b(float a, float b) { f2 v = {a, b}; return __builtin_bit_cast(unsigned, __builtin_convertvector(v, b2)); }
DEV unsigned asu(h2 v) { return __builtin_bit_cast(unsigned, v); }
DEV h2 ash(unsigned v) { return __builtin_bit_cast(h2, v); }
DEV float asf(unsigned v) { return __builtin_bit_cast(float, v); }
DEV unsigned asu(float v) { return __builtin_bit_cast(unsigned, v); }
DEV float bflo(unsigned v) { return asf(v << 16); }
DEV float bfhi(unsigned v) { return asf(v & 0xffff0000u); }
DEV h2 cmul(h2 a, h2 t) { h2 p = a.xx * t; h2 tt = {-t.y, t.x}; return __builtin_elementwise_fma(a.yy, tt, p); }
DEV h2 cmulconj(h2 a, h2 t) {
  h2 t1 = {t.x, -t.y}; h2 p = a.xx * t1; h2 tt = {-t.y, -t.x}; return __builtin_elementwise_fma(a.yy, tt, p);
}
DEV void radix4(h2 a0, h2 a1, h2 a2, h2 a3, h2& o0, h2& o1, h2& o2, h2& o3) {
  h2 t0 = a0 + a2, t1 = a0 - a2, t2 = a1 + a3, t3 = a1 - a3;
  o0 = t0 + t2; o2 = t0 - t2;
  o1 = t1 + (h2){t3.y, -t3.x};
  o3 = t1 + (h2){-t3.y, t3.x};
}
template <int DEN> DEV f2 wexp(int num) {
  float fr = (float)(num & (DEN - 1)) * (1.0f / DEN);
  return (f2){__builtin_amdgcn_cosf(fr), -__builtin_amdgcn_sinf(fr)};
}
DEV f4 mfma_h(u4 a, u4 b, f4 c) {
  return __builtin_amdgcn_mfma_f32_16x16x32_f16(__builtin_bit_cast(h8, a), __builtin_bit_cast(h8, b), c, 0, 0, 0);
}
DEV f4 mfma_b(u4 a, u4 b, f4 c) {
  return __builtin_amdgcn_mfma_f32_16x16x32_bf16(__builtin_bit_cast(b8, a), __builtin_bit_cast(b8, b), c, 0, 0, 0);
}
template <int CTRL> DEV unsigned dppu(unsigned x) { return (unsigned)__builtin_amdgcn_update_dpp(0, (int)x, CTRL, 0xf, 0xf, true); }
template <int CTRL> DEV float dppf(float x) { return asf(dppu<CTRL>(asu(x))); }
DEV u2 swap16(unsigned x) { unsigned y = x; asm volatile("" : "+v"(y)); auto r = __builtin_amdgcn_permlane16_swap(x, y, false, false); u2 o = {r[0], r[1]}; asm volatile("" : "+v"(o)); return o; }
DEV u2 swap32(unsigned x) { unsigned y = x; asm volatile("" : "+v"(y)); auto r = __builtin_amdgcn_permlane32_swap(x, y, false, false); u2 o = {r[0], r[1]}; asm volatile("" : "+v"(o)); return o; }
DEV float wave_sum(float x) {
  x += dppf<0xB1>(x); x += dppf<0x4E>(x); x += dppf<0x124>(x); x += dppf<0x128>(x);
  u2 a = swap16(asu(x)); x = asf(a.x) + asf(a.y);
  u2 b = swap32(asu(x)); x = asf(b.x) + asf(b.y);
  return x;
}
DEV unsigned pkmax(unsigned a, unsigned b) {
  typedef unsigned short s2 __attribute__((ext_vector_type(2)));
  return __builtin_bit_cast(unsigned, __builtin_elementwise_max(__builtin_bit_cast(s2, a), __builtin_bit_cast(s2, b)));
}
DEV unsigned wave_pkmax(unsigned x) {
  x = pkmax(x, dppu<0xB1>(x)); x = pkmax(x, dppu<0x4E>(x)); x = pkmax(x, dppu<0x124>(x)); x = pkmax(x, dppu<0x128>(x));
  u2 a = swap16(x); x = pkmax(a.x, a.y);
  u2 b = swap32(x); x = pkmax(b.x, b.y);
  return x;
}
DEV unsigned umax(unsigned a, unsigned b) { return a > b ? a : b; }
DEV unsigned umin(unsigned a, unsigned b) { return a < b ? a : b; }
DEV unsigned or4(u4 v) { return (v.x | v.y) | (v.z | v.w); }

typedef __amdgpu_buffer_rsrc_t rsrc_t;
DEV u4 ld16(rsrc_t r, unsigned v, unsigned s) { return __builtin_amdgcn_raw_buffer_load_b128(r, v, s, 16); }
DEV u2 ld8(rsrc_t r, unsigned v, unsigned s) { return __builtin_amdgcn_raw_buffer_load_b64(r, v, s, 16); }
DEV void st16(rsrc_t r, unsigned v, unsigned s, u4 d) {
  __builtin_amdgcn_raw_buffer_store_b128(d, r, v, s, 16);
  asm volatile("s_nop 1" :: "v"(d) : "memory");
}
DEV void st8(rsrc_t r, unsigned v, unsigned s, u2 d) { __builtin_amdgcn_raw_buffer_store_b64(d, r, v, s, 16); }
DEV u4 ldw(const unsigned short* p) { return __builtin_nontemporal_load((const u4a2*)p); }
DEV void wave_sync() {
  __builtin_amdgcn_fence(__ATOMIC_RELEASE, "wavefront");
  __builtin_amdgcn_wave_barrier();
  __builtin_amdgcn_fence(__ATOMIC_ACQUIRE, "wavefront");
}
#define MEMBAR() asm volatile("" ::: "memory")
#define DRAIN() asm volatile("s_waitcnt vmcnt(0)" ::: "memory")
#define SNOOZE(n) do { if constexpr ((n) > 0) __builtin_amdgcn_s_sleep((n) > 0 ? (n) : 0); } while (0)
DEV float opq(float v) { asm("" : "+v"(v)); return v; }
DEV unsigned and4(u4 v) { return (v.x & v.y) & (v.z & v.w); }
DEV u2 trread(const unsigned* p) { return __builtin_bit_cast(u2, __builtin_amdgcn_ds_read_tr16_b64_v4i16((LAS s4*)p)); }
DEV void pin(f4& v) { asm volatile("" : "+v"(v)); }
DEV void pin(u4& v) { asm volatile("" : "+v"(v)); }
DEV void pin(unsigned& v) { asm volatile("" : "+v"(v)); }
DEV void pin(float& v) { asm volatile("" : "+v"(v)); }

DEV int pos_to_n(int pi) {
  int n2 = pi >> 4;
  int gq = ((pi >> 2) & 3) ^ ((n2 >> 1) & 3);
  return 32 * (4 * gq + (pi & 3)) + n2;
}

#define POLL_LOOP(LOADS, OKEXPR)                                   \
  _Pragma("unroll 1") for (unsigned spins_ = 0;;) {                \
    LOADS;                                                         \
    if (__all(OKEXPR)) break;                                      \
    if (++spins_ > spin_limit) { spin_limit = 0; break; }          \
    MEMBAR();                                                      \
  }
#define SCHED() __builtin_amdgcn_sched_barrier(0)

__global__ void __attribute__((aligned(4096))) __launch_bounds__(NT, 2)
gfnet_kernel(const unsigned short* x, const unsigned short* awr, const unsigned short* awi, const unsigned short* bwr,
             const unsigned short* bwi, unsigned short* out, unsigned char* wsp
) {
  __shared__ __attribute__((aligned(16))) unsigned lds[L_TOTAL];
  const int t = threadIdx.x, lane = t & 63;
  const int wave = __builtin_amdgcn_readfirstlane(t >> 6);
  const int wg = blockIdx.x;
  const unsigned tstart = (unsigned)__builtin_amdgcn_s_memrealtime();
  const int slot = (wg & ~15) | ((wg & 7) << 1) | ((wg >> 3) & 1);
  const int row = pos_to_n(4 * (slot & 127) + (slot >> 7) + 2 * (wave >> 2)) + 512 * (wave & 3);
  const u4* const xrow = (const u4*)(x + row * 1024);
  const u4 xq0 = __builtin_nontemporal_load(xrow + lane), xq1 = __builtin_nontemporal_load(xrow + lane + 64);
  MEMBAR();
  SCHED();
  const rsrc_t ws = __builtin_amdgcn_make_buffer_rsrc(wsp, 0, WS_USED, 0x00020000);
  u2 epoch = ld8(ws, 0, OFF_E);
  MEMBAR();
  u64 pc; unsigned clen;
  asm volatile("s_getpc_b64 %0\n.Lgf_ref:\n\ts_mov_b32 %1, .Lgf_end-.Lgf_ref" : "=s"(pc), "=s"(clen));
  const __attribute__((address_space(1))) unsigned* cpa =
      (const __attribute__((address_space(1))) unsigned*)(pc + umin((unsigned)((wg >> 3) * 8 + wave + 4) * 128u, clen));
  const unsigned cpf = *cpa;
  MEMBAR();
  SCHED();
  const int l15 = lane & 15, g = lane >> 4;
  unsigned spin_limit = SPIN_LIMIT;
  const u4 sent4 = {SENT, SENT, SENT, SENT};
  const int g2 = (wg >> 3) & 3, sidx = (wg & 7) + 8 * (wg >> 5);
  const int jcol = (wg & 7) * 32 + (wg >> 3);
  const int q = wave & 3, wcol = wave >> 2;
  const unsigned v16 = t * 16;
  const f4 z4 = {0.f, 0.f, 0.f, 0.f};

  {
    const int G = wave < 2 ? 8 : 4, A = wave < 2 ? 4 * wave : 16 * (wave & 1);
    const int C = wave >= 6 ? 16 : 0, M = wave < 4 ? 32 : 1;
    const float sc = wave < 2 ? 0.25f : wave < 4 ? 0.125f : 1.0f;
    f2 w[4];
#pragma unroll
    for (int i = 0; i < 4; ++i) w[i] = wexp<1024>((G * g + A + i) * ((l15 + C) * M)) * sc;
    unsigned* ct = &lds[L_CT + lane * 4];
    if (wave < 2) {
      *(u2*)&ct[2 * wave] = (u2){asu(pack_h(w[0].x, w[1].x)), asu(pack_h(w[2].x, w[3].x))};
      *(u2*)&ct[256 + 2 * wave] = (u2){asu(pack_h(w[0].y, w[1].y)), asu(pack_h(w[2].y, w[3].y))};
    } else if (wave < 4) {
      *(u4*)&ct[(2 * wave - 2) * 256] = (u4){asu(pack_h(w[0].x, -w[0].y)), asu(pack_h(w[1].x, -w[1].y)), asu(pack_h(w[2].x, -w[2].y)), asu(pack_h(w[3].x, -w[3].y))};
      *(u4*)&ct[(2 * wave - 1) * 256] = (u4){asu(pack_h(w[0].y, w[0].x)), asu(pack_h(w[1].y, w[1].x)), asu(pack_h(w[2].y, w[2].x)), asu(pack_h(w[3].y, w[3].x))};
    } else {
      *(u4*)&ct[(wave + 2) * 256] = (u4){asu(pack_h(w[0].x, w[0].y)), asu(pack_h(w[1].x, w[1].y)), asu(pack_h(w[2].x, w[2].y)), asu(pack_h(w[3].x, w[3].y))};
    }
  }
  SCHED();
  const int woff = ((slot >> 6) + 4 * (slot & 15) + 64 * (16 * (lane >> 5) + 4 * ((slot >> 4) & 3) + ((lane >> 3) & 3))) * 513 + 1 + 8 * (lane & 7);
  const u4 war = ldw(awr + woff + 64 * wave), wai = ldw(awi + woff + 64 * wave);
  MEMBAR();
  __syncthreads();
  u4 ma1[2][2], m2[2][2][2], ta[2][2];
  {
    const u4* ct = (const u4*)&lds[L_CT + lane * 4];
    ma1[0][0] = ct[0]; ma1[0][1] = ct[64];
    m2[0][0][0] = ct[128]; m2[0][0][1] = ct[192]; m2[0][1][0] = ct[256]; m2[0][1][1] = ct[320];
    ta[0][0] = ct[384]; ta[1][0] = ct[448]; ta[0][1] = ct[512]; ta[1][1] = ct[576];
    ma1[1][0] = ma1[0][0] ^ 0x80000000u; ma1[1][1] = ma1[0][1] ^ 0x80000000u;
  }
  const unsigned vwm = (lane >> 2) * 16384 + (lane & 3) * 16;
  auto w_transpose = [&](u4 r, u4 i, u4 (&o)[2]) {
    unsigned* st = &lds[L_WP + wave * 8 * PITCH_W];
    u4* d = (u4*)&st[(lane >> 3) * PITCH_W + 8 * (lane & 7)];
    d[0] = (u4){__builtin_amdgcn_perm(i.x, r.x, 0x05040100u), __builtin_amdgcn_perm(i.x, r.x, 0x07060302u),
                __builtin_amdgcn_perm(i.y, r.y, 0x05040100u), __builtin_amdgcn_perm(i.y, r.y, 0x07060302u)};
    d[1] = (u4){__builtin_amdgcn_perm(i.z, r.z, 0x05040100u), __builtin_amdgcn_perm(i.z, r.z, 0x07060302u),
                __builtin_amdgcn_perm(i.w, r.w, 0x05040100u), __builtin_amdgcn_perm(i.w, r.w, 0x07060302u)};
    wave_sync();
    const unsigned* src = &st[((lane >> 1) & 1) * 4 * PITCH_W + 2 * (lane >> 2) + (lane & 1)];
#pragma unroll
    for (int h = 0; h < 2; ++h) o[h] = (u4){src[32 * h], src[32 * h + PITCH_W], src[32 * h + 2 * PITCH_W], src[32 * h + 3 * PITCH_W]};
  };
  const unsigned smsg = 32 * wave * 16384 + slot * 64;
  SCHED();
  asm volatile("" : "+v"(epoch));
  const u64 tag = (((u64)__builtin_amdgcn_readfirstlane(epoch.y) << 32) | __builtin_amdgcn_readfirstlane(epoch.x)) + 1;
  const u2 tag2 = {(unsigned)tag, (unsigned)(tag >> 32)};
  u4 wbr, wbi;
  SCHED();
  {
    const float x0 = bflo(__builtin_amdgcn_readfirstlane(xq0.x));
    *(u4*)&lds[L_X + wave * 512 + lane * 4] = xq0; *(u4*)&lds[L_X + wave * 512 + 256 + lane * 4] = xq1;
    f2 d[8], s2 = {0.f, 0.f}, q2 = {0.f, 0.f};
#pragma unroll
    for (int j = 0; j < 8; ++j) {
      const unsigned v = j < 4 ? xq0[j & 3] : xq1[j & 3];
      d[j] = (f2){bflo(v), bfhi(v)} - x0;
      s2 += d[j];
      q2 = __builtin_elementwise_fma(d[j], d[j], q2);
    }
    const float se = wave_sum(s2.x), so = wave_sum(s2.y), qq = wave_sum(q2.x + q2.y);
    const float md = (se + so) * (1.0f / 1024);
    const float var = __builtin_fmaxf(__builtin_fmaf(-md, md, qq * (1.0f / 1024)), 0.f) + 1e-5f;
    const float r = __builtin_amdgcn_rsqf(var) * 16.f;
    const float off = -md * r;
    unsigned* img = &lds[L_TILE + wave * 512];
#pragma unroll
    for (int h = 0; h < 2; ++h) {
      u4 v;
#pragma unroll
      for (int j = 0; j < 4; ++j) v[j] = asu(__builtin_convertvector(__builtin_elementwise_fma(d[4 * h + j], (f2){r, r}, (f2){off, off}), h2));
      *(u4*)&img[256 * h + 16 * (lane >> 2) + 4 * ((lane & 3) ^ (2 * (lane >> 5)))] = v;
    }
    wave_sync();
    u4 xa[2];
#pragma unroll
    for (int e = 0; e < 2; ++e) {
      const unsigned* src = &img[128 * g + 16 * (l15 >> 2) + 2 * (l15 & 3) + 8 * (e ^ (g & 1))];
      const u2 lo = trread(src), hi = trread(src + 64);
      xa[e] = (u4){lo.x, lo.y, hi.x, hi.y};
    }
    u4 hh[2][2];
#pragma unroll
    for (int k = 0; k < 2; ++k)
#pragma unroll
      for (int e = 0; e < 2; ++e) {
        f4 dr = mfma_h(xa[e], ma1[k][0], z4), di = mfma_h(xa[e], ma1[k][1], z4);
#pragma unroll
        for (int i = 0; i < 4; ++i) hh[e][k][i] = asu(cmul(pack_h(dr[i], di[i]), ash(ta[e][k][i])));
      }
#pragma unroll
    for (int k = 0; k < 2; ++k) {
      f4 orr = mfma_h(hh[0][k], m2[0][0][0], z4), oi = mfma_h(hh[0][k], m2[0][0][1], z4);
      orr = mfma_h(hh[1][k], m2[0][1][0], orr); oi = mfma_h(hh[1][k], m2[0][1][1], oi);
      u4 o;
#pragma unroll
      for (int i = 0; i < 4; ++i) o[i] = asu(pack_h(orr[i], oi[i])) & ~1u;
      *(u4*)&lds[L_ST + wave * PITCH_A + 36 * l15 + 16 * k + 4 * g] = o;
    }
    if (lane == 0) {
      lds[L_ST + wave * PITCH_A + 576] = asu(pack_h((se - so) * r * (0.70710678f / 32), 0.f)) & ~1u;
      lds[L_MISC + wave] = asu(var * r * (1.0f / 256));
    }
  }
  u4 wat[2];
  w_transpose(war, wai, wat);
  st16(ws, vwm, OFF_WA + smsg, wat[0]); st16(ws, vwm, OFF_WA + smsg + 16 * 16384, wat[1]);
  __syncthreads();
  const unsigned gate = umax(DLY.u1, (unsigned)__builtin_amdgcn_s_memrealtime() - tstart + DLY.b2);
#pragma unroll
  for (int h = 0; h < 2; ++h) {
    const int k = 64 * wave + 32 * h + 2 * (lane >> 2) + 1 + (lane & 1);
    const unsigned* src = &lds[L_ST + ((lane >> 1) & 1) * 4 * PITCH_A + k + 4 * (k >> 5)];
    st16(ws, vwm, OFF_U1 + smsg + h * 16 * 16384, (u4){src[0], src[PITCH_A], src[2 * PITCH_A], src[3 * PITCH_A]});
  }
  if (lane < 2) st16(ws, lane * 16, OFF_S1 + (wave * 256 + slot) * 32, *(const u4*)&lds[L_MISC + 4 * lane]);
  MEMBAR();
  st16(ws, lane < 32 ? OFF_P + (sidx * 4 + g2) * 4096 + wave * 64 + (lane >> 2) * 512 + (lane & 3) * 16
                     : OFF_R + ((umin((lane >> 3) - 4, 2u) * REP + wave) * 4 + g2) * 8192 + sidx * 128 + (lane & 7) * 16, 0, sent4);
  MEMBAR();
  SCHED();
  asm volatile("" :: "v"(cpf));
  DRAIN();
  if (lane == 0) st8(ws, 0, OFF_F1 + (wave * 256 + slot) * FST, tag2);
  MEMBAR();

  const unsigned inbox = jcol * 16384, inflag = (wg & 7) * 256 * FST;
  auto amax4 = [&](const u4& a) { const unsigned M = 0x7fff7fffu; return pkmax(pkmax(a.x & M, a.y & M), pkmax(a.z & M, a.w & M)); };
  auto flag_bad = [&](const u2& f) { return (f.x ^ tag2.x) | (f.y ^ tag2.y); };
  auto w_fetch = [&](u4 (&a)[4], unsigned fl, unsigned base, int tt) {
    u2 f;
    POLL_LOOP(f = ld8(ws, (tt - 256) * FST, fl + inflag), flag_bad(f) == 0);
    MEMBAR();
#pragma unroll
    for (int i = 0; i < 4; ++i) a[i] = ld16(ws, (tt & 63) * 16, base + inbox + (wave - 4) * 4096 + i * 1024);
    MEMBAR();
  };
  auto w_stage = [&](const u4 (&a)[4], int tt) {
    unsigned m = 0;
#pragma unroll
    for (int i = 0; i < 4; ++i) {
      *(u4*)&lds[L_WST + ((wave - 4) * 64 + i * 16 + ((tt & 63) >> 2)) * 20 + (tt & 3) * 4] = a[i];
      m = pkmax(m, amax4(a[i]));
    }
    m = wave_pkmax(umax(m & 0xffffu, m >> 16) << ((tt & 1) * 16));
    if (lane == 0) lds[L_MISC + 12 + wave] = m;
  };
  auto w_convert = [&](u4 (&wv)[2], float& us, int tt) {
    const u4 ma = *(const u4*)&lds[L_MISC + 16], mb = *(const u4*)&lds[L_MISC + 12];
    const u4* src = (const u4*)&lds[L_WST + (q * 64 + (tt & 63)) * 20 + wcol * 4];
    const u4 v0 = src[0], v1 = src[2];
    const unsigned m = pkmax(pkmax(pkmax(ma.x, ma.y), pkmax(ma.z, ma.w)), pkmax(pkmax(mb.x, mb.y), pkmax(mb.z, mb.w)));
    const unsigned e0 = umin(umax((m & 0xffffu) >> 7, 4u), 251u), e1 = umin(umax(m >> 23, 4u), 251u);
    const float sc = asf((252u - (wcol ? e1 : e0)) << 23);
    us = asf((((tt & 1) ? e1 : e0) - 3u) << 23);
#pragma unroll
    for (int i = 0; i < 4; ++i) {
      wv[0][i] = asu(pack_h(bflo(v0[i]) * sc, bfhi(v0[i]) * sc));
      wv[1][i] = asu(pack_h(bflo(v1[i]) * sc, bfhi(v1[i]) * sc));
    }
  };

  u4 ua, ub, sig;
  u4 wv[2], wo[2];
  float us, uso;
  asm volatile("" : "=v"(wo[0]), "=v"(wo[1]), "=v"(uso));
  asm volatile(".p2align 12,,320");
  while ((unsigned)__builtin_amdgcn_s_memrealtime() - tstart < gate) __builtin_amdgcn_s_sleep(1);
  u4 wa[2];
  {
    u2 f;
    POLL_LOOP(f = ld8(ws, (wave * 16 + l15 + 128 * (g & 1)) * FST, OFF_F1 + inflag), flag_bad(f) == 0);
    MEMBAR();
    ua = ld16(ws, v16, OFF_U1 + inbox); ub = ld16(ws, v16, OFF_U1 + inbox + 8192);
    MEMBAR();
    SCHED();
  }
  {
    u4 fr, fi, tw0, tw1;
#pragma unroll
    for (int i = 0; i < 4; ++i) {
      const int X = wcol ? l15 : 4 * g + i, F = q + 4 * (wcol ? 4 * g + i : l15);
      const f2 w = wexp<64>(X * F) * 0.25f;
      fr[i] = asu(pack_h(w.x, -w.y)); fi[i] = asu(pack_h(w.y, w.x));
      const f2 a = wexp<2048>(X * F), b = wexp<2048>((X + 16) * F);
      tw0[i] = asu(pack_h(a.x, a.y)); tw1[i] = asu(pack_h(b.x, b.y));
      if (i == 1) {
        pin(fr); pin(fi); pin(tw0); pin(tw1);
        SCHED();
        MEMBAR();
        wa[0] = ld16(ws, v16, OFF_WA + inbox); wa[1] = ld16(ws, v16, OFF_WA + inbox + 8192);
        sig = ld16(ws, ((t & 1) * 128 + (t >> 2)) * 32 + ((t >> 1) & 1) * 16, OFF_S1 + (wg & 7) * 8192);
        MEMBAR();
        SCHED();
      }
    }
    u4* cl = (u4*)&lds[L_CL + (q * 8 + wcol * 4) * 256 + lane * 4];
    cl[0] = fr; cl[64] = fi; cl[128] = tw0; cl[192] = tw1;
  }
  u4 m1[2], m3[2], redA;
  unsigned t1[2][4], t2[2][4];
  {
    const u4 si = {0u, 0x80008000u, 0u, 0x80008000u};
#pragma unroll
    for (int k = 0; k < 2; ++k)
#pragma unroll
      for (int e = 0; e < 2; ++e) { m2[1][k][e] = m2[0][k][e] ^ si; pin(m2[1][k][e]); }
#pragma unroll
    for (int i = 0; i < 4; ++i) redA[i] = ((l15 & 7) >> 1) == i ? ((l15 & 1) ? 0x3F800000u : 0x3F80u) : 0u;
    pin(redA);
  }
  unsigned own_i = L_TILE + (t & 1) * TILE_COL + (t >> 1) * 2;
  unsigned tile_i = L_TILE + q * TILE_Q + wcol * TILE_COL + l15 * 16 + 4 * (g ^ ((l15 >> 1) & 3));
  unsigned vp = (t >> 3) * 16384 + ((t >> 1) & 3) * 16;
  unsigned vr = ((l15 >> 2) + 4 * (g >> 1)) * 32768 + (l15 & 3) * 32 + (g & 1) * 16;
  float imask = ((jcol == NWG - 1) && (t & 1)) ? 0.f : -1.f;
  pin(own_i); pin(tile_i); pin(vp); pin(vr); pin(imask);
  unsigned* const own = &lds[own_i];
  unsigned* const tile = &lds[tile_i];
  auto butterflies = [&](const h2 (&u)[2][4]) {
    h2 c[2][4];
    radix4(u[0][0], u[0][1], u[0][2], u[0][3], c[0][0], c[0][1], c[0][2], c[0][3]);
    radix4(u[1][0], u[1][1], u[1][2], u[1][3], c[1][0], c[1][1], c[1][2], c[1][3]);
#pragma unroll
    for (int k = 0; k < 4; ++k) *(u2*)&own[k * TILE_Q] = (u2){asu(c[0][k]), asu(c[1][k])};
  };
  f4 dr[2], di[2];
  auto forward = [&]() {
    u4 hh[2];
#pragma unroll
    for (int i = 0; i < 4; ++i) { pin(t1[0][i]); pin(t1[1][i]); }
    const u4 a0 = *(const u4*)&tile[0], a1 = *(const u4*)&tile[256];
    dr[0] = mfma_h(a0, m1[0], z4); di[0] = mfma_h(a0, m1[1], z4);
    dr[1] = mfma_h(a1, m1[0], z4); di[1] = mfma_h(a1, m1[1], z4);
#pragma unroll
    for (int b = 0; b < 2; ++b)
#pragma unroll
      for (int i = 0; i < 4; ++i) hh[b][i] = asu(cmul(pack_h(dr[b][i], di[b][i]), ash(t1[b][i])));
#pragma unroll
    for (int k = 0; k < 2; ++k) { dr[k] = mfma_h(m2[k][0][0], hh[0], z4); di[k] = mfma_h(m2[k][0][1], hh[0], z4); }
#pragma unroll
    for (int k = 0; k < 2; ++k) { dr[k] = mfma_h(m2[k][1][0], hh[1], dr[k]); di[k] = mfma_h(m2[k][1][1], hh[1], di[k]); }
  };
  auto p_init = [&](int n) {
#pragma unroll
    for (int i = 0; i < 4; ++i) st16(ws, v16, OFF_P + (n * 256 + sidx * 4 + g2) * 4096 + i * 1024, sent4);
  };
  SCHED();
  pin(ua); pin(ub);
  MEMBAR();
  wbr = ldw(bwr + woff + 64 * wave); wbi = ldw(bwi + woff + 64 * wave);
  MEMBAR();
  {
    const h2 u[2][4] = {{ash(ua[0]), ash(ua[1]), ash(ua[2]), ash(ua[3])}, {ash(ub[0]), ash(ub[1]), ash(ub[2]), ash(ub[3])}};
    butterflies(u);
  }
  __syncthreads();
  {
    const u4* cl = (const u4*)&lds[L_CL + q * 8 * 256 + lane * 4];
    m1[0] = cl[0]; m1[1] = cl[64]; m3[0] = cl[256]; m3[1] = cl[320];
    const u4 a0 = cl[128], a1 = cl[192], b0 = cl[384], b1 = cl[448];
#pragma unroll
    for (int i = 0; i < 4; ++i) { t1[0][i] = a0[i]; t1[1][i] = a1[i]; t2[0][i] = b0[i]; t2[1][i] = b1[i]; }
  }
  forward();
  float zr[2][4], zi[2][4];
#pragma unroll
  for (int m = 0; m < 4; ++m) {
    const float sa = dppf<0xA0>(asf(sig[m])), sb = dppf<0xF5>(asf(sig[m]));
    zr[0][m] = opq(__builtin_fmaf((float)ash(ua[m]).x, sa, 0.f)); zi[0][m] = opq(__builtin_fmaf((float)ash(ua[m]).y, sa, 0.f));
    zr[1][m] = opq(__builtin_fmaf((float)ash(ub[m]).x, sb, 0.f)); zi[1][m] = opq(__builtin_fmaf((float)ash(ub[m]).y, sb, 0.f));
  }
  SCHED();
  {
    *(u4*)&lds[L_WST + (t >> 2) * 20 + (t & 3) * 4] = wa[0];
    *(u4*)&lds[L_WST + (128 + (t >> 2)) * 20 + (t & 3) * 4] = wa[1];
    unsigned m = pkmax(amax4(wa[0]), amax4(wa[1]));
    m = wave_pkmax(umax(m & 0xffffu, m >> 16) << ((t & 1) * 16));
    if (lane == 0) lds[L_MISC + 12 + wave] = m;
  }
  __syncthreads();
  w_convert(wv, us, t);
  {
    u4 wbt[2];
    w_transpose(wbr, wbi, wbt);
    st16(ws, vwm, OFF_WB + smsg, wbt[0]); st16(ws, vwm, OFF_WB + smsg + 16 * 16384, wbt[1]);
    if (wave == 0) p_init(1);
  }

#pragma unroll 1
  for (int L = 0;; ++L) {
    {
      u4 hh[2], vv[2];
#pragma unroll
      for (int i = 0; i < 4; ++i) { pin(t2[0][i]); pin(t2[1][i]); }
#pragma unroll
      for (int k = 0; k < 2; ++k)
#pragma unroll
        for (int i = 0; i < 4; ++i) vv[k][i] = asu(cmulconj(pack_h(dr[k][i], di[k][i]), ash(wv[k][i])));
#pragma unroll
      for (int b = 0; b < 2; ++b) { dr[b] = mfma_h(vv[0], m2[b][0][0], z4); di[b] = mfma_h(vv[0], m2[b][0][1], z4); }
#pragma unroll
      for (int b = 0; b < 2; ++b) { dr[b] = mfma_h(vv[1], m2[b][1][0], dr[b]); di[b] = mfma_h(vv[1], m2[b][1][1], di[b]); }
#pragma unroll
      for (int b = 0; b < 2; ++b)
#pragma unroll
        for (int i = 0; i < 4; ++i) hh[b][i] = asu(cmul(pack_h(dr[b][i], di[b][i]), ash(t2[b][i])));
#pragma unroll
      for (int b = 0; b < 2; ++b) { dr[b] = mfma_h(m3[0], hh[b], z4); di[b] = mfma_h(m3[1], hh[b], z4); }
#pragma unroll
      for (int b = 0; b < 2; ++b) {
        u4 o;
#pragma unroll
        for (int i = 0; i < 4; ++i) o[i] = asu(pack_h(dr[b][i], di[b][i]));
        *(u4*)&tile[b * 256] = o;
      }
    }
    if (L == 0) {
      DRAIN();
      if (lane == 0) st8(ws, 0, OFF_F3 + (wave * 256 + slot) * FST, tag2);
      MEMBAR();
    }
    const float usi = us * imask;
    __syncthreads();
    {
      u2 e[4];
#pragma unroll
      for (int k = 0; k < 4; ++k) e[k] = *(const u2*)&own[k * TILE_Q];
      h2 y[2][4];
      radix4(ash(e[0].x), ash(e[1].x), ash(e[2].x), ash(e[3].x), y[0][0], y[0][1], y[0][2], y[0][3]);
      radix4(ash(e[0].y), ash(e[1].y), ash(e[2].y), ash(e[3].y), y[1][0], y[1][1], y[1][2], y[1][3]);
#pragma unroll
      for (int s = 0; s < 2; ++s)
#pragma unroll
        for (int m = 0; m < 4; ++m) {
          zr[s][m] = opq(__builtin_fmaf((float)y[s][m].x, us, zr[s][m]));
          zi[s][m] = opq(__builtin_fmaf((float)y[s][m].y, usi, zi[s][m]));
        }
    }
    if (L == 3) break;
    float zsr[2][4], zsi[2][4];
    {
      float p[2][4];
#pragma unroll
      for (int s = 0; s < 2; ++s)
#pragma unroll
        for (int m = 0; m < 4; ++m) {
          const float sq = opq(__builtin_fmaf(zr[s][m], zr[s][m], opq(zi[s][m] * zi[s][m])));
          p[s][m] = opq(sq + dppf<0xB1>(sq));
        }
      const unsigned M = 0x7fff7fffu;
      u4 pv = {pack_b(p[0][0], p[0][1]) & M, pack_b(p[0][2], p[0][3]) & M, pack_b(p[1][0], p[1][1]) & M, pack_b(p[1][2], p[1][3]) & M};
      if (!(t & 1)) st16(ws, vp, OFF_P + (L * 256 + g2) * 4096 + sidx * 64, pv);
      MEMBAR();
      { const u4 a0 = wv[0], a1 = wv[1]; wv[0] = wo[0]; wv[1] = wo[1]; wo[0] = a0; wo[1] = a1; const float tmp = us; us = uso; uso = tmp; }
      pin(wv[0]); pin(wv[1]); pin(wo[0]); pin(wo[1]); pin(us); pin(uso);
#pragma unroll
      for (int s = 0; s < 2; ++s)
#pragma unroll
        for (int m = 0; m < 4; ++m) { zsr[s][m] = zr[s][m]; zsi[s][m] = zi[s][m]; pin(zsr[s][m]); pin(zsi[s][m]); }
      if (L == 2) {
        pin(ua); pin(ub);
#pragma unroll
        for (int s = 0; s < 2; ++s)
#pragma unroll
          for (int m = 0; m < 4; ++m) {
            const float sg = s ? dppf<0xF5>(asf(sig[m])) : dppf<0xA0>(asf(sig[m]));
            const h2 u0 = ash(s ? ub[m] : ua[m]);
            zr[s][m] = __builtin_fmaf(-(float)u0.x, sg, zr[s][m]); zi[s][m] = __builtin_fmaf(-(float)u0.y, sg, zi[s][m]);
            pin(zr[s][m]); pin(zi[s][m]);
          }
        int lo = lane;
        asm volatile("" : "+v"(lo));
        const int lo15 = lo & 15, go = lo >> 4;
        const int bb = 8 * (lo15 >> 2) + 4 * (wave & 1) + (lo15 & 3);
        const int F = ((wave & 6) == 2 ? lo15 : bb) * (wave < 4 ? 32 : 1);
        const int K = (wave == 3 || wave >= 6) ? 16 : 0;
        const float sc = wave < 4 ? 0.25f : 1.0f;
        f2 w[4];
#pragma unroll
        for (int i = 0; i < 4; ++i) w[i] = wexp<1024>((K + 4 * go + i) * F) * sc;
        unsigned* ct = &lds[L_CT + lo * 4];
        if (wave < 4) {
          *(u4*)&ct[(wave < 2 ? 2 * wave : wave + 2) * 256] = (u4){pack_b(w[0].x, w[0].y), pack_b(w[1].x, w[1].y), pack_b(w[2].x, w[2].y), pack_b(w[3].x, w[3].y)};
          if (wave < 2) *(u4*)&ct[(2 * wave + 1) * 256] = (u4){pack_b(-w[0].y, w[0].x), pack_b(-w[1].y, w[1].x), pack_b(-w[2].y, w[2].x), pack_b(-w[3].y, w[3].x)};
        } else {
          *(f4*)&ct[(2 * wave - 2) * 256] = (f4){w[0].x, w[1].x, w[2].x, w[3].x};
          *(f4*)&ct[(2 * wave - 1) * 256] = (f4){-w[0].y, -w[1].y, -w[2].y, -w[3].y};
        }
      }
      if (wave == 0) {
        if (L == 0) SNOOZE(DLY.s0); else if (L == 1) SNOOZE(DLY.h1); else SNOOZE(DLY.h1b);
        u4 v[4];
        const unsigned pb = OFF_P + (L * 256 + sidx * 4 + g2) * 4096;
        POLL_LOOP(v[0] = ld16(ws, v16, pb); v[1] = ld16(ws, v16, pb + 1024); v[2] = ld16(ws, v16, pb + 2048); v[3] = ld16(ws, v16, pb + 3072),
                  (int)(or4(v[0]) | or4(v[1]) | or4(v[2]) | or4(v[3])) >= 0);
        f4 acc = z4;
#pragma unroll
        for (int i = 0; i < 4; ++i) acc = mfma_b(redA, v[i], acc);
        u4 o;
#pragma unroll
        for (int i = 0; i < 4; ++i) { float s = acc[i]; s = opq(s + dppf<0x124>(s)); s = opq(s + dppf<0x128>(s)); o[i] = asu(s) & 0x7fffffffu; }
        st16(ws, vr, OFF_R + (L * REP * 4 + g2) * 8192 + sidx * 128, o);
        MEMBAR();
      }
      if (L == 0) {
        if (wg == 0 && t == 128) st8(ws, 0, OFF_E, tag2);
        MEMBAR();
        int tt = t;
        asm volatile("" : "+v"(tt));
        if (wave >= 4) {
          SNOOZE(DLY.wb);
          u4 a[4];
          w_fetch(a, OFF_F3, OFF_WB, tt);
          w_stage(a, tt);
        } else if (lane == 0) {
          lds[L_MISC + 12 + wave] = 0u;
        }
        __syncthreads();
        w_convert(wv, us, tt);
        SNOOZE(DLY.s1);
      } else {
        __builtin_amdgcn_s_barrier();
        SNOOZE(DLY.h2);
      }
    }
    {
      u4 v[4];
      const unsigned rb = OFF_R + ((L * REP + (wg & (REP - 1))) * 4) * 8192;
      POLL_LOOP(v[0] = ld16(ws, v16, rb); v[1] = ld16(ws, v16, rb + 8192); v[2] = ld16(ws, v16, rb + 16384); v[3] = ld16(ws, v16, rb + 24576),
                (int)(or4(v[0]) | or4(v[1]) | or4(v[2]) | or4(v[3])) >= 0);
      h2 u[2][4];
#pragma unroll
      for (int m = 0; m < 4; ++m) {
        const float sum = opq(opq(asf(v[0][m]) + asf(v[1][m])) + opq(asf(v[2][m]) + asf(v[3][m])));
        const float r = __builtin_amdgcn_rsqf(__builtin_fmaf(sum, 2.0f / 1024 / 256, 1e-5f / 256));
        const float ra = dppf<0xA0>(r), rbb = dppf<0xF5>(r);
        u[0][m] = pack_h(opq(zsr[0][m] * ra), opq(zsi[0][m] * ra));
        u[1][m] = pack_h(opq(zsr[1][m] * rbb), opq(zsi[1][m] * rbb));
      }
      butterflies(u);
    }
    __syncthreads();
    if (wave == 0 && L == 0) p_init(2);
    forward();
  }
#pragma unroll
  for (int s = 0; s < 2; ++s) {
    u4 d;
#pragma unroll
    for (int m = 0; m < 4; ++m) {
      d[m] = __builtin_amdgcn_perm(asu(zi[s][m]), asu(zr[s][m]), 0x07060302u) | 0x00010001u;
    }
    st16(ws, v16, OFF_U1 + inbox + 8192 * s, d);
  }
  MEMBAR();
  {
    if (lane == 0) lds[L_ST + wave * PITCH_B] = 0u;
    const int qd = lane >> 2;
    const int j0 = 16 * ((qd >> 1) & 3) + (qd & 1) + 2 * (qd >> 3) + 4 * (wave & 3) + 64 * (wave >> 2);
    const unsigned vz = j0 * 16384 + (t & 3) * 16;
    u4 mb1[2][2], mb2[2][2];
    f4 tbr[2][2], tbi[2][2], xf[2][2];
    const u4 xres[2] = {*(const u4*)&lds[L_X + wave * 512 + l15 * 16 + g * 4], *(const u4*)&lds[L_X + wave * 512 + 256 + l15 * 16 + g * 4]};
    {
      const u4* ct = (const u4*)&lds[L_CT + lane * 4];
      mb1[0][0] = ct[0]; mb1[0][1] = ct[64]; mb1[1][0] = ct[128]; mb1[1][1] = ct[192];
      mb2[0][0] = ct[256]; mb2[0][1] = ct[320];
      const u4 sg = {0u, 0x80008000u, 0u, 0x80008000u};
      mb2[1][0] = mb2[0][0] ^ sg; mb2[1][1] = mb2[0][1] ^ sg;
#pragma unroll
      for (int k = 0; k < 2; ++k)
#pragma unroll
        for (int b = 0; b < 2; ++b) {
          tbr[k][b] = __builtin_bit_cast(f4, ct[(6 + 4 * k + 2 * b) * 64]); tbi[k][b] = __builtin_bit_cast(f4, ct[(7 + 4 * k + 2 * b) * 64]);
          xf[k][b] = (f4){bflo(xres[k][2 * b]), bfhi(xres[k][2 * b]), bflo(xres[k][2 * b + 1]), bfhi(xres[k][2 * b + 1])};
          pin(tbr[k][b]); pin(tbi[k][b]); pin(xf[k][b]); pin(mb1[k][b]); pin(mb2[k][b]);
        }
    }
    float nys = g == 0 ? ((l15 & 1) ? -0.17677670f : 0.17677670f) : 0.f;
    int kpos[2];
#pragma unroll
    for (int h = 0; h < 2; ++h) {
      const int k = 2 * (j0 + 128 * h) + 1 + (t & 1);
      kpos[h] = L_ST + ((t >> 1) & 1) * 4 * PITCH_B + ((k == 512) ? 640 : (k & 31) * 20 + (k >> 5));
      asm volatile("" : "+v"(kpos[h]));
    }
    pin(nys);
    SNOOZE(DLY.dz);
    u4 v[2];
    POLL_LOOP(v[0] = ld16(ws, vz, OFF_U1 + slot * 64); v[1] = ld16(ws, vz, OFF_U1 + slot * 64 + 128 * 16384), (and4(v[0]) & and4(v[1]) & 1u) != 0);
#pragma unroll
    for (int h = 0; h < 2; ++h) {
      unsigned* dst = &lds[kpos[h]];
      dst[0] = v[h].x; dst[PITCH_B] = v[h].y; dst[2 * PITCH_B] = v[h].z; dst[3 * PITCH_B] = v[h].w;
    }
    __syncthreads();
    const unsigned* src = &lds[L_ST + wave * PITCH_B];
    const u4 a0 = *(const u4*)&src[l15 * 20 + 4 * g], a1 = *(const u4*)&src[(16 + l15) * 20 + 4 * g];
    const f4 cn = {bflo(src[640]) * nys, 0.f, 0.f, 0.f};
    u4 gg[2][2];
#pragma unroll
    for (int k = 1; k >= 0; --k)
#pragma unroll
      for (int b = 0; b < 2; ++b) {
        f4 gr = mfma_b(k ? a1 : a0, mb1[b][0], k ? z4 : cn), gi = mfma_b(k ? a1 : a0, mb1[b][1], z4);
#pragma unroll
        for (int i = 0; i < 4; i += 2) {
          const f2 cr = {tbr[k][b][i], tbr[k][b][i + 1]}, ci = {tbi[k][b][i], tbi[k][b][i + 1]};
          const f2 r2 = {gr[i], gr[i + 1]}, i2 = {gi[i], gi[i + 1]};
          const f2 re = __builtin_elementwise_fma(r2, cr, -(i2 * ci)), im = __builtin_elementwise_fma(r2, ci, i2 * cr);
          gg[k][b][i] = pack_b(re.x, im.x); gg[k][b][i + 1] = pack_b(re.y, im.y);
        }
      }
#pragma unroll
    for (int a = 0; a < 2; ++a) {
      u4 o;
#pragma unroll
      for (int b = 0; b < 2; ++b) {
        f4 y = mfma_b(gg[1][b], mb2[a][1], xf[a][b]);
        y = mfma_b(gg[0][b], mb2[a][0], y);
        o[2 * b] = pack_b(y[0], y[1]);
        o[2 * b + 1] = pack_b(y[2], y[3]);
      }
      __builtin_amdgcn_raw_buffer_store_b128(o, __builtin_amdgcn_make_buffer_rsrc(out, 0, 2048 * 1024 * 2, 0x00020000), row * 2048 + (a * 64 + l15 * 4 + g) * 16, 0, 16);
    }
    asm volatile(".Lgf_end:");
  }
}

extern "C" void kernel_launch(void* const* d_in, const int* in_sizes, int n_in, void* d_out, int out_size, void* d_ws,
                              size_t ws_size, hipStream_t stream) {
  gfnet_kernel<<<NWG, NT, 0, stream>>>((const unsigned short*)d_in[0], (const unsigned short*)d_in[1],
                                       (const unsigned short*)d_in[2], (const unsigned short*)d_in[4],
                                       (const unsigned short*)d_in[5], (unsigned short*)d_out, (unsigned char*)d_ws);
}
```
